```python
import torch

def _softmax(x, axis=-1):
    return torch.softmax(x, dim=axis)
def _gelu(x):
    return torch.nn.functional.gelu(x, approximate="tanh")
def _silu(x):
    return torch.nn.functional.silu(x)
def _sigmoid(x):
    return torch.sigmoid(x)
def _layernorm(x, axis=-1, eps=1e-5):
    m = x.mean(dim=axis, keepdim=True)
    v = x.var(dim=axis, keepdim=True, unbiased=False)
    return (x - m) / torch.sqrt(v + eps)
def _scatter_add(tgt, idx, src, axis=0):
    return tgt.clone().index_add_(axis, idx, src)
def _segment_sum(src, seg, n):
    out = torch.zeros((n,) + src.shape[1:], dtype=src.dtype)
    return out.index_add_(0, seg, src)
def _pad(x, pw):
    flat = tuple(v for pair in reversed(pw) for v in pair)
    return torch.nn.functional.pad(x, flat)

kernel_name = 'standalone__paged_block_table_kv_radixattention__decode'

def setup_inputs():

    _w = (lambda a: (lambda t: t.to(torch.bfloat16) if t.is_floating_point() else t)(torch.as_tensor(a)))
    B, H, Hkv, Dh = 1, 32, 16, 128
    Lk = 8192
    bs = 16
    nb = max(2, Lk // bs)
    P = max(1, nb // 2)
    n_phys = P + B * (nb - P)
    shared = torch.arange(P, dtype=torch.int64)
    priv = P + torch.arange(B * (nb - P), dtype=torch.int64).reshape(B, nb - P)
    bt = torch.cat([torch.tile(shared[None, :], (B, 1)), priv], dim=1)
    ctx = nb * bs - (torch.arange(1, B + 1, dtype=torch.int64) * 2654435761) % max(1, (nb * bs - P * bs) // 2)
    return {
        "q": _w((lambda *s: torch.randn(*s, dtype=torch.bfloat16) * 0.8)(B, H, Dh)),
        "k_cache": _w((lambda *s: torch.randn(*s, dtype=torch.bfloat16) * 0.8)(n_phys, bs, Hkv, Dh)),
        "v_cache": _w((lambda *s: torch.randn(*s, dtype=torch.bfloat16) * 0.05)(n_phys, bs, Hkv, Dh)),
        "block_table": _w(bt.to(torch.int64)),
        "ctx_lens": _w(torch.as_tensor(ctx).to(torch.int64)),
    }

def reference(q, k_cache, v_cache, block_table, ctx_lens):
    B, H, Dh = q.shape
    n_phys, bs, Hkv, _ = k_cache.shape
    nb = block_table.shape[1]
    Lk = nb * bs
    rep = H // Hkv
    k = k_cache[block_table].reshape(B, Lk, Hkv, Dh)
    v = v_cache[block_table].reshape(B, Lk, Hkv, Dh)
    k = (lambda x, n, axis: torch.repeat_interleave(x, n, dim=axis))(k, rep, axis=2)
    v = (lambda x, n, axis: torch.repeat_interleave(x, n, dim=axis))(v, rep, axis=2)
    scores = torch.einsum("bhd,bkhd->bhk", q, k) * (Dh ** -0.5)
    pos = torch.arange(Lk)
    valid = pos[None, :] < ctx_lens[:, None]
    scores = torch.where(valid[:, None, :], scores, -1e9)
    p = _softmax(scores, axis=-1)
    return torch.einsum("bhk,bkhd->bhd", p, v)
```

```cpp
#include <hip/hip_runtime.h>
#include <hip/hip_bf16.h>
#include <stdint.h>

constexpr int HKV = 16, D = 128, BS = 16, NB = 512;
constexpr int NW = 4, NTHR = NW * 64, NWG = 256, NS = 16, NSTEP = 32;
constexpr unsigned PAGE_BYTES = BS * HKV * D * 2;
constexpr unsigned TOK_BYTES = HKV * D * 2;
constexpr unsigned RSRC_FLAGS = 0x00020000u;
constexpr unsigned OOB = 0x7f000000u;
constexpr unsigned WS_EP = 0, WS_GW = 16 * 128, WS_GO = WS_GW + 32 * 16 * 16, WS_END = WS_GO + 16 * 16 * 16 * 16 * 8;
constexpr int WIN = 6;
constexpr int JA = 1;
constexpr int JH = JA + 2;
constexpr int J0 = JH + 1 + WIN;
constexpr int JL = 10;
constexpr int JW = 18;
constexpr int JW2 = JW + WIN + 1;

typedef unsigned u32x4 __attribute__((ext_vector_type(4)));
typedef float f32x4 __attribute__((ext_vector_type(4)));
typedef float f32x2 __attribute__((ext_vector_type(2)));
typedef __bf16 bf16x8 __attribute__((ext_vector_type(8)));
typedef __bf16 bf16x2 __attribute__((ext_vector_type(2)));
typedef __attribute__((address_space(1))) unsigned gu32;
#define SB() __builtin_amdgcn_sched_barrier(0)
constexpr int vmcnt_imm(int n) { return (n & 15) | ((n >> 4) << 14) | (7 << 4) | (15 << 8); }

__device__ __forceinline__ f32x4 mfma16(u32x4 a, u32x4 b, f32x4 c) {
  return __builtin_amdgcn_mfma_f32_16x16x32_bf16(__builtin_bit_cast(bf16x8, a), __builtin_bit_cast(bf16x8, b), c, 0, 0, 0);
}
__device__ __forceinline__ unsigned cvtpk(float lo, float hi) {
  return __builtin_bit_cast(unsigned, __builtin_convertvector(f32x2{lo, hi}, bf16x2));
}
template <int CTRL> __device__ __forceinline__ float dpp(float x) {
  return __builtin_bit_cast(float, __builtin_amdgcn_mov_dpp(__builtin_bit_cast(int, x), CTRL, 0xf, 0xf, true));
}
__device__ __forceinline__ float xrow_max(float x) {
  auto s = __builtin_amdgcn_permlane16_swap(__float_as_uint(x), __float_as_uint(x), false, false);
  x = fmaxf(__uint_as_float(s[0]), __uint_as_float(s[1]));
  auto t = __builtin_amdgcn_permlane32_swap(__float_as_uint(x), __float_as_uint(x), false, false);
  return fmaxf(__uint_as_float(t[0]), __uint_as_float(t[1]));
}
__device__ __forceinline__ float xrow_sum(float x) {
  auto s = __builtin_amdgcn_permlane16_swap(__float_as_uint(x), __float_as_uint(x), false, false);
  x = __uint_as_float(s[0]) + __uint_as_float(s[1]);
  auto t = __builtin_amdgcn_permlane32_swap(__float_as_uint(x), __float_as_uint(x), false, false);
  return __uint_as_float(t[0]) + __uint_as_float(t[1]);
}
template <int I> struct IC { static constexpr int v = I; };
template <int B, int E, class F> __device__ __forceinline__ void static_for(F&& f) {
  if constexpr (B < E) { f(IC<B>{}); static_for<B + 1, E>(f); }
}
template <int N> __device__ __forceinline__ void wait_vm() { __builtin_amdgcn_s_waitcnt(vmcnt_imm(N)); }
__device__ __forceinline__ u32x4 ld_granules(__amdgpu_buffer_rsrc_t r, unsigned off) {
  asm volatile("" : "+v"(off) :: "memory");
  return __builtin_amdgcn_raw_buffer_load_b128(r, off, 0, 16);
}

__global__ void __launch_bounds__(NTHR, 2) attn_decode(const unsigned short* __restrict__ kc, unsigned kbytes, unsigned kflags,
                                                       char* ws, const unsigned short* __restrict__ q,
                                                       const unsigned short* __restrict__ vc, const int* __restrict__ bt,
                                                       __hip_bfloat16* __restrict__ out, const int* __restrict__ lens) {
  __shared__ __attribute__((aligned(16))) float lds[NW * 256 + 16 + NB];
  float* lo = lds;
  float* lm = lds + NW * 256;
  float* ll = lm + 8;
  unsigned* cnt = (unsigned*)(lds + NW * 256 + 16);
  const int split = blockIdx.x & 15, kvh = blockIdx.x >> 4;
  const int tid = threadIdx.x, lane = tid & 63, g = lane >> 4, c = lane & 15;
  *(uint2*)(cnt + 2 * tid) = make_uint2(0u, 0u);
  SB();
  const int wave = __builtin_amdgcn_readfirstlane(tid >> 6);
  const int hsel = (c >> 2) & 1;
  const int slot = wave * 16 + split;

  unsigned base = slot * (4 * TOK_BYTES) + kvh * (D * 2);
  unsigned laneK = (c >> 2) * TOK_BYTES + (4 * g + (c & 3)) * 16;
  const unsigned laneV = g * TOK_BYTES + c * 16;
  unsigned qoff = (2 * kvh + hsel) * (D * 2) + (4 * g + (c & 3)) * 16, epoff = WS_EP + kvh * 128;
  asm volatile("s_waitcnt lgkmcnt(0)" : "+s"(base), "+v"(laneK), "+v"(qoff), "+v"(epoff) : "s"(out) : "memory");
  SB();
  int len = lens[0];
  const u32x4 Q = *(const u32x4*)((const char*)q + qoff);
  const int2 bte = *(const int2*)(bt + 2 * tid);
  const unsigned ep = __hip_atomic_load((gu32*)(ws + epoff), __ATOMIC_RELAXED, __HIP_MEMORY_SCOPE_AGENT);
  SB();
  const __amdgpu_buffer_rsrc_t rk = __builtin_amdgcn_make_buffer_rsrc((void*)kc, 0, kbytes, kflags);
  const __amdgpu_buffer_rsrc_t rv = __builtin_amdgcn_make_buffer_rsrc((void*)vc, 0, NB * PAGE_BYTES, RSRC_FLAGS);
  u32x4 X[2 * NSTEP];
  unsigned live = 0;
  auto issue = [&](auto I, auto W) __attribute__((always_inline)) {
    constexpr int j = decltype(I)::v, w = decltype(W)::v;
    unsigned so = base + (j % NSTEP) * (16 * PAGE_BYTES);
    if constexpr (j >= J0) so = ((live >> (j % NSTEP)) & 1u) ? so : OOB;
    if constexpr (w >= 0) { asm volatile("" : "+s"(so)); wait_vm<w>(); }
    if constexpr (j < 2 * NSTEP) X[j] = __builtin_amdgcn_raw_buffer_load_b128(j < NSTEP ? rk : rv, j < NSTEP ? laneK : laneV, so, 2  );
  };
  static_for<0, WIN>([&](auto I) __attribute__((always_inline)) { issue(I, IC<-1>{}); });
  SB();
  __builtin_amdgcn_s_barrier();
  SB();
  float c2 = 0.088388347648318440f * 1.4426950408889634f;
  float sc[NSTEP], bw[2], m = -INFINITY;
  f32x4 dk[NSTEP];
#define SCORE(j) sc[j] = ((dk[j][0] + dpp<0x55>(dk[j][1])) + dpp<0xAA>(dk[j][2])) + dpp<0xFF>(dk[j][3])
#define WEIGHT(j) do { const float b_ = dpp<0x150 + ((j) & 15)>(bw[(j) >> 4]); \
    sc[j] = (b_ == -INFINITY) ? -INFINITY : fmaf(sc[j], c2, b_); m = fmaxf(m, sc[j]); } while (0)
  static_for<0, NSTEP + 1>([&](auto J) __attribute__((always_inline)) {
    constexpr int j = decltype(J)::v, i = j - 1;
    if constexpr (j < NSTEP) {
      issue(IC<j + WIN>{}, IC<WIN - 1>{});
      SB();
      if constexpr (j == JA) {
        const bool nolen = len <= 0;
        len = nolen ? NB * BS : min(len, NB * BS);
        const int r0 = len - 32 * tid, r1 = r0 - 16;
        __hip_atomic_fetch_add(cnt + (bte.x & (NB - 1)), r0 >= 16 ? 1u : (unsigned)max(r0, 0) << 16, __ATOMIC_RELAXED, __HIP_MEMORY_SCOPE_WORKGROUP);
        __hip_atomic_fetch_add(cnt + (bte.y & (NB - 1)), r1 >= 16 ? 1u : (unsigned)max(r1, 0) << 16, __ATOMIC_RELAXED, __HIP_MEMORY_SCOPE_WORKGROUP);
        if (nolen) c2 = 0.f;
      }
      dk[j] = mfma16(X[j], Q, f32x4{0.f, 0.f, 0.f, 0.f});
    }
    if constexpr (i >= 0) SCORE(i);
    if constexpr (j == JH) {
      __syncthreads();
      const unsigned cv = cnt[16 * (lane & 31) + (slot >> 2)];
      live = (unsigned)__ballot((cv & 0xffffu) != 0u || (cv >> 16) > 4u * (slot & 3));
#pragma unroll
      for (int u = 0; u < 2; ++u) {
        const unsigned cu = cnt[16 * (c + 16 * u) + (slot >> 2)];
        bw[u] = __builtin_amdgcn_logf((float)((cu & 0xffffu) + ((unsigned)(4 * (slot & 3) + g) < (cu >> 16) ? 1u : 0u)));
      }
    }
    if constexpr (i >= JH) { WEIGHT(i); if constexpr (i - JH < JH) WEIGHT(i - JH); }
    SB();
  });
  m = xrow_max(m);
  if ((lane & 0x3b) == 0) lm[hsel * NW + wave] = m;
  __syncthreads();
  float MC;
  {
    const f32x4 t = *(const f32x4*)(lm + hsel * NW);
    const float M = fmaxf(fmaxf(t[0], t[1]), fmaxf(t[2], t[3]));
    MC = (M == -INFINITY) ? 0.f : M;
  }
  const unsigned tag = (ep + 0x9E3779B9u) | 1u;
  const bool cons = wave == 0;
  const __amdgpu_buffer_rsrc_t rw = __builtin_amdgcn_make_buffer_rsrc((void*)ws, 0, WS_END, RSRC_FLAGS);
  const u32x4 rws = {(unsigned)(uintptr_t)ws, (unsigned)((uintptr_t)ws >> 32) & 0xffffu, WS_END, RSRC_FLAGS};
  const __amdgpu_buffer_rsrc_t ro = __builtin_amdgcn_make_buffer_rsrc((void*)out, 0, 2 * HKV * D * 2, RSRC_FLAGS);
  const u32x4 ones = {0x3f803f80u, 0x3f803f80u, 0x3f803f80u, 0x3f803f80u};
  f32x4 lacc = {0.f, 0.f, 0.f, 0.f};
  f32x4 O[8];
#pragma unroll
  for (int jj = 0; jj < 8; ++jj) O[jj] = f32x4{0.f, 0.f, 0.f, 0.f};
  u32x4 P[4], B[4][8], gw[2];
  float rinv = 0.f, e[2];
  bool wok = false;
  const unsigned offw = cons ? WS_GW + ((kvh * 2 + (split >> 3)) * NS + (lane >> 3)) * 16 : OOB;
  unsigned offo0 = WS_GO + (kvh * NS + split) * (NS * 128) + lane * 16, offo1 = offo0 + 1024;
  unsigned offp = (c & 1) ? OOB : WS_GO + ((kvh * NS + (tid >> 4)) * NS + split) * 128 + c * 8;
  unsigned offout = g == 0 ? (kvh * 256 + split * 16 + 2 * (c & 7) + (c >> 3)) * 2 : OOB;
  unsigned offe = (split == 0 && lane == 0) ? WS_EP + kvh * 128 : OOB;
  unsigned lwr = (wave * 256 + (g & 1) * 128 + 8 * c + (g >> 1) * 4) * 4, lrd = tid * 4;
  auto wvalid = [&]() __attribute__((always_inline)) { return (bool)__all((gw[0][1] == tag) & (gw[0][3] == tag) & (gw[1][1] == tag) & (gw[1][3] == tag)); };
  auto weights = [&]() __attribute__((always_inline)) {
    float W = fmaxf(__uint_as_float(gw[0][0]), __uint_as_float(gw[1][0]));
    W = xrow_max(fmaxf(W, dpp<0x128>(W)));
    e[0] = __builtin_amdgcn_exp2f(__uint_as_float(gw[0][0]) - W);
    e[1] = __builtin_amdgcn_exp2f(__uint_as_float(gw[1][0]) - W);
    float den = e[0] + e[1];
    den = __builtin_amdgcn_rcpf(xrow_sum(den + dpp<0x128>(den)));
    e[0] *= den; e[1] *= den;
  };
  static_for<0, NSTEP>([&](auto J) __attribute__((always_inline)) {
    constexpr int j = decltype(J)::v;
    constexpr int extra = ((j > JW && j <= JW + WIN) ? 2 : 0) + ((j > JL && j <= JL + WIN) ? 1 : 0);
    if constexpr (NSTEP + j + WIN < 2 * NSTEP) issue(IC<NSTEP + j + WIN>{}, IC<WIN - 1 + extra>{});
    if constexpr (j == JW) { gw[0] = ld_granules(rw, offw); gw[1] = ld_granules(rw, offw + 128); }
    SB();
    if constexpr (j < 8) {
#pragma unroll
      for (int h = 0; h < 2; ++h)
        P[j >> 1][(j & 1) * 2 + h] = cvtpk(__builtin_amdgcn_exp2f(sc[4 * j + 2 * h] - MC), __builtin_amdgcn_exp2f(sc[4 * j + 2 * h + 1] - MC));
      if constexpr (j & 1) lacc = mfma16(P[j >> 1], ones, lacc);
    }
    if constexpr (j == 8) { if ((lane & 0x2f) == 0) ll[g * NW + wave] = lacc[0]; }
    if constexpr (j == JL) {
      __syncthreads();
      const int qh = wave >> 1;
      const f32x4 lw = *(const f32x4*)(ll + qh * NW);
      const float L = (lw[0] + lw[1]) + (lw[2] + lw[3]);
      rinv = (L > 0.f) ? __builtin_amdgcn_rcpf(L) : 0.f;
      const unsigned wl = __float_as_uint((L > 0.f) ? MC + __builtin_amdgcn_logf(L) : -INFINITY);
      __builtin_amdgcn_raw_buffer_store_b128(u32x4{wl, tag, wl, tag}, rw,
                                             ((wave & 1) == 0 && lane == 4 * qh) ? WS_GW + ((kvh * 2 + qh) * NS + split) * 16 : OOB, 0, 16);
    }
    if constexpr (j == JW2) {
      wok = wvalid();
      weights();
      asm volatile("" : "+v"(e[0]), "+v"(e[1]), "+v"(offp), "+v"(offout), "+v"(lwr), "+v"(lrd), "+v"(offo0), "+v"(offo1), "+v"(offe));
    }
    if constexpr (j & 1) {
#pragma unroll
      for (int jj = 0; jj < 8; ++jj)
        B[j >> 3][jj][(j & 7) >> 1] = __builtin_amdgcn_perm(X[NSTEP + j][jj >> 1], X[NSTEP + j - 1][jj >> 1], (jj & 1) ? 0x07060302u : 0x05040100u);
    }
    if constexpr (j >= 8 && (j & 7) < 4) {
#pragma unroll
      for (int jj = 2 * (j & 7); jj < 2 * (j & 7) + 2; ++jj) O[jj] = mfma16(P[(j >> 3) - 1], B[(j >> 3) - 1][jj], O[jj]);
    }
    if constexpr (j == NSTEP - 1) {
#pragma unroll
      for (int jj = 0; jj < 8; ++jj) O[jj] = mfma16(P[3], B[3][jj], O[jj]);
    }
    SB();
  });
  f32x4 ov;
#pragma unroll
  for (int k = 0; k < 4; ++k) ov[k] = (g >= 2) ? O[k + 4][0] : O[k][0];
  *(f32x4*)((char*)lo + lwr) = ov;
  __syncthreads();
  const float* lr = (const float*)((const char*)lo + lrd);
  const float on = ((lr[0] + lr[256]) + (lr[512] + lr[768])) * rinv;
  __builtin_amdgcn_raw_buffer_store_b128(u32x4{__float_as_uint(on), tag, __float_as_uint(dpp<0xB1>(on)), tag}, rw, offp, 0, 16);
  if (!cons) return;
  if (!wok) {
#pragma unroll 1
    for (unsigned spins = 0;; ++spins) {
      gw[0] = ld_granules(rw, offw); gw[1] = ld_granules(rw, offw + 128);
      if (wvalid()) break;
      if (spins > (1u << 18)) { gw[0][0] = 0x7fc00000u; break; }
    }
    weights();
  }
  float o00 = 0.f, o01 = 0.f, o10 = 0.f, o11 = 0.f;
  unsigned long long p0 = ~0ull, p1 = ~0ull, h0, h1, h2, h3;
  unsigned left = 1u << 18, oob = OOB;
#define LD2(R0, R1) "buffer_load_dwordx4 " R0 ", %[f0], %[rs], 0 offen sc1\n\tbuffer_load_dwordx4 " R1 ", %[f1], %[rs], 0 offen sc1\n\t"
#define CHK(A0, A1, A2, A3, B0, B1, B2, B3) \
    "s_waitcnt vmcnt(4)\n\t" \
    "v_cmp_eq_u32_e64 %[h0], %[tag], " A1 "\n\tv_cmp_eq_u32_e64 %[h1], %[tag], " A3 "\n\t" \
    "v_cmp_eq_u32_e64 %[h2], %[tag], " B1 "\n\tv_cmp_eq_u32_e64 %[h3], %[tag], " B3 "\n\t" \
    "s_and_b64 %[h0], %[h0], %[h1]\n\ts_and_b64 %[h2], %[h2], %[h3]\n\t" \
    "v_cndmask_b32_e64 %[o00], %[o00], " A0 ", %[h0]\n\tv_cndmask_b32_e64 %[o01], %[o01], " A2 ", %[h0]\n\t" \
    "v_cndmask_b32_e64 %[o10], %[o10], " B0 ", %[h2]\n\tv_cndmask_b32_e64 %[o11], %[o11], " B2 ", %[h2]\n\t" \
    "s_andn2_b64 %[p0], %[p0], %[h0]\n\ts_andn2_b64 %[p1], %[p1], %[h2]\n\t" \
    "s_or_b64 vcc, %[p0], %[p1]\n\t" \
    "s_cbranch_scc0 9f\n\t" \
    "v_cndmask_b32_e64 %[f0], %[f0], %[oob], %[h0]\n\tv_cndmask_b32_e64 %[f1], %[f1], %[oob], %[h2]\n\t"
  asm volatile(
    "buffer_store_dword %[tag], %[fe], %[rs], 0 offen sc1\n\t"
    LD2("v[136:139]", "v[140:143]")
    "s_sleep 4\n\t"
    LD2("v[144:147]", "v[148:151]")
    "s_sleep 4\n\t"
    LD2("v[152:155]", "v[156:159]")
    "1:\n\t"
    CHK("v136", "v137", "v138", "v139", "v140", "v141", "v142", "v143")
    LD2("v[136:139]", "v[140:143]")
    CHK("v144", "v145", "v146", "v147", "v148", "v149", "v150", "v151")
    LD2("v[144:147]", "v[148:151]")
    CHK("v152", "v153", "v154", "v155", "v156", "v157", "v158", "v159")
    LD2("v[152:155]", "v[156:159]")
    "s_sub_u32 %[left], %[left], 1\n\t"
    "s_cbranch_scc0 1b\n\t"
    "v_mov_b32 %[o00], 0x7fc00000\n"
    "9:\n\t"
    : [o00] "+v"(o00), [o01] "+v"(o01), [o10] "+v"(o10), [o11] "+v"(o11), [f0] "+v"(offo0), [f1] "+v"(offo1),
      [p0] "+s"(p0), [p1] "+s"(p1), [h0] "=&s"(h0), [h1] "=&s"(h1), [h2] "=&s"(h2), [h3] "=&s"(h3), [left] "+s"(left)
    : [tag] "v"(tag), [oob] "v"(oob), [rs] "s"(rws), [fe] "v"(offe)
    : "memory", "vcc", "scc", "v136", "v137", "v138", "v139", "v140", "v141", "v142", "v143", "v144", "v145", "v146", "v147",
      "v148", "v149", "v150", "v151", "v152", "v153", "v154", "v155", "v156", "v157", "v158", "v159");
  const float n0 = fmaf(e[0], o00, e[1] * o10), n1 = fmaf(e[0], o01, e[1] * o11);
  const bool hi = (lane & 8) != 0;
  const float num = xrow_sum((hi ? n1 : n0) + dpp<0x128>(hi ? n0 : n1));
  __builtin_amdgcn_raw_buffer_store_b16(__builtin_bit_cast(unsigned short, (__bf16)num), ro, offout, 0, 16);
}

extern "C" void kernel_launch(void* const* d_in, const int* in_sizes, int n_in, void* d_out, int out_size, void* d_ws, size_t ws_size,
                              hipStream_t stream) {
  if (n_in != 5 || in_sizes[0] != 2 * HKV * D || in_sizes[1] != NB * BS * HKV * D || in_sizes[2] != in_sizes[1] || in_sizes[3] != NB ||
      in_sizes[4] != 1 || out_size != 2 * HKV * D || ws_size < WS_END) return;
  attn_decode<<<NWG, NTHR, 0, stream>>>((const unsigned short*)d_in[1], NB * PAGE_BYTES, RSRC_FLAGS, (char*)d_ws, (const unsigned short*)d_in[0],
                                        (const unsigned short*)d_in[2], (const int*)d_in[3], (__hip_bfloat16*)d_out, (const int*)d_in[4]);
}
```
